```python
import torch

def _softmax(x, axis=-1):
    return torch.softmax(x, dim=axis)
def _gelu(x):
    return torch.nn.functional.gelu(x, approximate="tanh")
def _silu(x):
    return torch.nn.functional.silu(x)
def _sigmoid(x):
    return torch.sigmoid(x)
def _layernorm(x, axis=-1, eps=1e-5):
    m = x.mean(dim=axis, keepdim=True)
    v = x.var(dim=axis, keepdim=True, unbiased=False)
    return (x - m) / torch.sqrt(v + eps)
def _scatter_add(tgt, idx, src, axis=0):
    return tgt.clone().index_add_(axis, idx, src)
def _segment_sum(src, seg, n):
    out = torch.zeros((n,) + src.shape[1:], dtype=src.dtype)
    return out.index_add_(0, seg, src)
def _pad(x, pw):
    flat = tuple(v for pair in reversed(pw) for v in pair)
    return torch.nn.functional.pad(x, flat)

kernel_name = 'standalone__fused_bias_dropout_add__prompt'

def setup_inputs():

    _w = (lambda a: (lambda t: t.to(torch.bfloat16) if t.is_floating_point() else t)(torch.as_tensor(a)))
    B, S, D = 2, 2048, 1024
    keep = 0.9
    mask = (torch.rand(B, S, D) < keep).to(float)
    return {
        "x": _w((lambda *s: torch.randn(*s, dtype=torch.bfloat16) * 0.05)(B, S, D)),
        "bias": _w((lambda *s: torch.randn(*s, dtype=torch.bfloat16) * 0.05)(D)),
        "mask": _w(mask),
        "residual": _w((lambda *s: torch.randn(*s, dtype=torch.bfloat16) * 0.05)(B, S, D)),
    }

def reference(x, bias, mask, residual):
    keep = 0.9
    return residual + (x + bias) * mask * (1.0 / keep)
```

```cpp
#include <hip/hip_runtime.h>
#include <stdint.h>

typedef __attribute__((ext_vector_type(4))) unsigned int u32x4;
typedef __attribute__((ext_vector_type(2))) float f32x2;
typedef __attribute__((ext_vector_type(2))) __bf16 bf16x2;

__device__ __forceinline__ float lo(unsigned v) { return __uint_as_float(v << 16); }
__device__ __forceinline__ float hi(unsigned v) { return __uint_as_float(v & 0xffff0000u); }
__device__ __forceinline__ unsigned pk(float a, float b) {
  bf16x2 r; r.x = (__bf16)a; r.y = (__bf16)b;
  return __builtin_bit_cast(unsigned, r);
}

__device__ __forceinline__ unsigned op2(unsigned x, unsigned b, unsigned m, unsigned r) {
  const float s = (float)(1.0 / 0.9);
  unsigned t = pk(lo(x) + lo(b), hi(x) + hi(b));
  t = pk(lo(t) * lo(m), hi(t) * hi(m));
  t = pk(lo(t) * s, hi(t) * s);
  return pk(lo(r) + lo(t), hi(r) + hi(t));
}

__global__ __launch_bounds__(256) void fbda_kernel(const u32x4* __restrict__ x, const u32x4* __restrict__ bias,
    const u32x4* __restrict__ mask, const u32x4* __restrict__ res, u32x4* __restrict__ out, int nvec, int dvec) {
  int i = blockIdx.x * 256 + threadIdx.x;
  if (i >= nvec) return;
  u32x4 vx = x[i], vm = mask[i], vr = res[i];
  u32x4 vb = bias[i % dvec];
  u32x4 o;
  o.x = op2(vx.x, vb.x, vm.x, vr.x);
  o.y = op2(vx.y, vb.y, vm.y, vr.y);
  o.z = op2(vx.z, vb.z, vm.z, vr.z);
  o.w = op2(vx.w, vb.w, vm.w, vr.w);
  out[i] = o;
}

extern "C" void kernel_launch(void* const* d_in, const int* in_sizes, int n_in,
                              void* d_out, int out_size, void* d_ws, size_t ws_size,
                              hipStream_t stream) {
  int n = in_sizes[0];
  int d = in_sizes[1];
  int nvec = n / 8;
  fbda_kernel<<<(nvec + 255) / 256, 256, 0, stream>>>((const u32x4*)d_in[0], (const u32x4*)d_in[1],
     (const u32x4*)d_in[2], (const u32x4*)d_in[3], (u32x4*)d_out, nvec, d / 8);
}
```

```cpp
#include <hip/hip_runtime.h>
#include <stdint.h>

typedef __attribute__((ext_vector_type(4))) unsigned int u32x4;
typedef __attribute__((ext_vector_type(2))) float f32x2;
typedef __attribute__((ext_vector_type(2))) __bf16 bf16x2;

static __device__ __forceinline__ f32x2 up(unsigned v) {
  f32x2 r;
  r.x = __uint_as_float(v << 16);
  r.y = __uint_as_float(v & 0xffff0000u);
  return r;
}
static __device__ __forceinline__ unsigned pk(f32x2 a) {
  bf16x2 r = __builtin_convertvector(a, bf16x2);
  return __builtin_bit_cast(unsigned, r);
}
static __device__ __forceinline__ f32x2 rnd(f32x2 a) { return up(pk(a)); }

#define KEEP_SCALE ((float)(1.0 / 0.9))

constexpr int kBlock = 512;

__global__ __launch_bounds__(kBlock) void fbda_fast(const char* __restrict__ x, const char* __restrict__ mask,
                                                    const char* __restrict__ res, const char* __restrict__ bias,
                                                    char* __restrict__ out) {
  const unsigned lane_off = threadIdx.x * 16u;
  const unsigned off = blockIdx.x * (kBlock * 16u) + lane_off;
  const u32x4 vx = *reinterpret_cast<const u32x4*>(x + off);
  __builtin_amdgcn_sched_barrier(0);
  const u32x4 vb = *reinterpret_cast<const u32x4*>(bias + (lane_off & 2047u));
  __builtin_amdgcn_sched_barrier(0);
  const u32x4 vm = __builtin_nontemporal_load(reinterpret_cast<const u32x4*>(mask + off));
  const u32x4 vr = *reinterpret_cast<const u32x4*>(res + off);
  __builtin_amdgcn_sched_barrier(0);
  f32x2 t0 = rnd(up(vx.x) + up(vb.x));
  f32x2 t1 = rnd(up(vx.y) + up(vb.y));
  f32x2 t2 = rnd(up(vx.z) + up(vb.z));
  f32x2 t3 = rnd(up(vx.w) + up(vb.w));
  __builtin_amdgcn_sched_barrier(0);
  t0 = rnd(rnd(t0 * up(vm.x)) * KEEP_SCALE);
  t1 = rnd(rnd(t1 * up(vm.y)) * KEEP_SCALE);
  t2 = rnd(rnd(t2 * up(vm.z)) * KEEP_SCALE);
  t3 = rnd(rnd(t3 * up(vm.w)) * KEEP_SCALE);
  __builtin_amdgcn_sched_barrier(0);
  u32x4 o;
  o.x = pk(up(vr.x) + t0);
  o.y = pk(up(vr.y) + t1);
  o.z = pk(up(vr.z) + t2);
  o.w = pk(up(vr.w) + t3);
  *reinterpret_cast<u32x4*>(out + off) = o;
}

__global__ __launch_bounds__(256) void fbda_generic(const unsigned short* __restrict__ x,
                                                    const unsigned short* __restrict__ mask,
                                                    const unsigned short* __restrict__ res,
                                                    const unsigned short* __restrict__ bias,
                                                    unsigned short* __restrict__ out, long long n, long long d) {
  const long long stride = (long long)gridDim.x * 256;
  for (long long i = (long long)blockIdx.x * 256 + threadIdx.x; i < n; i += stride) {
    f32x2 t = rnd(up(x[i]) + up(bias[i % d]));
    t = rnd(rnd(t * up(mask[i])) * KEEP_SCALE);
    out[i] = (unsigned short)(pk(up(res[i]) + t) & 0xffffu);
  }
}

extern "C" void kernel_launch(void* const* d_in, const int* in_sizes, int n_in, void* d_out, int out_size,
                              void* d_ws, size_t ws_size, hipStream_t stream) {
  const long long n = in_sizes[0];
  const long long d = in_sizes[1];
  const uintptr_t align = (uintptr_t)d_in[0] | (uintptr_t)d_in[1] | (uintptr_t)d_in[2] | (uintptr_t)d_in[3] | (uintptr_t)d_out;
  if (d == 1024 && n > 0 && n % (kBlock * 8) == 0 && n * 2 <= 0xffffffffLL && (align & 15) == 0) {
    fbda_fast<<<(unsigned)(n / (kBlock * 8)), kBlock, 0, stream>>>((const char*)d_in[0], (const char*)d_in[2],
                                                                 (const char*)d_in[3], (const char*)d_in[1],
                                                                 (char*)d_out);
  } else if (n > 0 && d > 0) {
    long long blocks = (n + 255) / 256;
    if (blocks > 8192) blocks = 8192;
    fbda_generic<<<(unsigned)blocks, 256, 0, stream>>>((const unsigned short*)d_in[0], (const unsigned short*)d_in[2],
                                                       (const unsigned short*)d_in[3], (const unsigned short*)d_in[1],
                                                       (unsigned short*)d_out, n, d);
  }
}
```
